```python
import torch

def _softmax(x, axis=-1):
    return torch.softmax(x, dim=axis)
def _gelu(x):
    return torch.nn.functional.gelu(x, approximate="tanh")
def _silu(x):
    return torch.nn.functional.silu(x)
def _sigmoid(x):
    return torch.sigmoid(x)
def _layernorm(x, axis=-1, eps=1e-5):
    m = x.mean(dim=axis, keepdim=True)
    v = x.var(dim=axis, keepdim=True, unbiased=False)
    return (x - m) / torch.sqrt(v + eps)
def _scatter_add(tgt, idx, src, axis=0):
    return tgt.clone().index_add_(axis, idx, src)
def _segment_sum(src, seg, n):
    out = torch.zeros((n,) + src.shape[1:], dtype=src.dtype)
    return out.index_add_(0, seg, src)
def _pad(x, pw):
    flat = tuple(v for pair in reversed(pw) for v in pair)
    return torch.nn.functional.pad(x, flat)

kernel_name = 'standalone__softmax_full_bidir__decode'

def setup_inputs():

    _w = (lambda a: (lambda t: t.to(torch.bfloat16) if t.is_floating_point() else t)(torch.as_tensor(a)))
    B, H, Sq, Skv, Dh = 8, 32, 1, 4096, 128
    return {
        "q": _w((lambda *s: torch.randn(*s, dtype=torch.bfloat16) * 0.8)(B, H, Sq, Dh)),
        "k": _w((lambda *s: torch.randn(*s, dtype=torch.bfloat16) * 0.8)(B, H, Skv, Dh)),
        "v": _w((lambda *s: torch.randn(*s, dtype=torch.bfloat16) * 0.05)(B, H, Skv, Dh)),
    }

def reference(q, k, v):
    scale = q.shape[-1] ** -0.5
    scores = torch.einsum('bhqd,bhkd->bhqk', q, k) * scale
    attn = _softmax(scores, axis=-1)
    return torch.einsum('bhqk,bhkd->bhqd', attn, v)
```

```cpp
#include <hip/hip_runtime.h>
#include <hip/hip_bf16.h>
#include <stdint.h>

typedef unsigned u32x4 __attribute__((ext_vector_type(4)));
typedef float f32x4 __attribute__((ext_vector_type(4)));
typedef __bf16 bf16x2 __attribute__((ext_vector_type(2)));

#define NW 16
#define TL 4
#define DEPTH 2

constexpr int D = 128, SKV = 4096;
constexpr int TILE_KEYS = 4 * TL;
constexpr int NT = SKV / TILE_KEYS / NW;
static_assert(NT % DEPTH == 0 && NT >= DEPTH, "tiles per wave must be a multiple of the ring depth");

__device__ __forceinline__ float dot2(unsigned k, unsigned q, float acc) {
  return __builtin_amdgcn_fdot2_f32_bf16(__builtin_bit_cast(bf16x2, k), __builtin_bit_cast(bf16x2, q), acc, false);
}
template <int CTRL> __device__ __forceinline__ float dpp(float x) {
  return __builtin_bit_cast(float, __builtin_amdgcn_mov_dpp(__builtin_bit_cast(int, x), CTRL, 0xf, 0xf, true));
}
constexpr int XOR1 = 0xB1, XOR2 = 0x4E, XOR7 = 0x141, XOR8 = 0x128;
__device__ __forceinline__ float row16_sum(float x) {
  x += dpp<XOR1>(x); x += dpp<XOR2>(x); x += dpp<XOR7>(x); x += dpp<XOR8>(x);
  return x;
}
__device__ __forceinline__ float xrow_max(float x) {
  auto s = __builtin_amdgcn_permlane16_swap(__float_as_uint(x), __float_as_uint(x), false, false);
  x = fmaxf(__uint_as_float(s[0]), __uint_as_float(s[1]));
  auto t = __builtin_amdgcn_permlane32_swap(__float_as_uint(x), __float_as_uint(x), false, false);
  return fmaxf(__uint_as_float(t[0]), __uint_as_float(t[1]));
}
__device__ __forceinline__ float xrow_sum(float x) {
  auto s = __builtin_amdgcn_permlane16_swap(__float_as_uint(x), __float_as_uint(x), false, false);
  x = __uint_as_float(s[0]) + __uint_as_float(s[1]);
  auto t = __builtin_amdgcn_permlane32_swap(__float_as_uint(x), __float_as_uint(x), false, false);
  return __uint_as_float(t[0]) + __uint_as_float(t[1]);
}
__device__ __forceinline__ u32x4 ldnt(const u32x4* p) { return __builtin_nontemporal_load(p); }

__global__ void __launch_bounds__(NW * 64) attn_decode_kernel(const u32x4* __restrict__ q, const u32x4* __restrict__ k,
                                                              const u32x4* __restrict__ v, unsigned* __restrict__ out) {
  __shared__ __attribute__((aligned(16))) float lds[NW * (D + 4)];
  const int head = blockIdx.x;
  const int tid = threadIdx.x, lane = tid & 63, wave = __builtin_amdgcn_readfirstlane(tid >> 6);
  const int r = lane >> 4, c = lane & 15;
  constexpr float C2 = 0.088388347648318440f * 1.4426950408889634f;

  const u32x4 qv = q[head * 16 + c];
  const u32x4* kb = k + (size_t)head * (SKV * 16) + wave * (TL * 64) + lane;
  const u32x4* vb = v + (size_t)head * (SKV * 16) + wave * (TL * 64) + lane;
  constexpr int TSTRIDE = NW * TL * 64;

  u32x4 Kr[DEPTH][TL], Vr[DEPTH][TL];
#pragma unroll
  for (int s = 0; s < DEPTH; ++s) {
#pragma unroll
    for (int i = 0; i < TL; ++i) Kr[s][i] = ldnt(kb + s * TSTRIDE + i * 64);
#pragma unroll
    for (int i = 0; i < TL; ++i) Vr[s][i] = ldnt(vb + s * TSTRIDE + i * 64);
  }

  float m = -INFINITY, l = 0.f, acc[8];
#pragma unroll
  for (int j = 0; j < 8; ++j) acc[j] = 0.f;

  for (int t0 = 0; t0 < NT; t0 += DEPTH) {
    const bool more = t0 + DEPTH < NT;
#pragma unroll
    for (int s = 0; s < DEPTH; ++s) {
      float x[TL];
#pragma unroll
      for (int i = 0; i < TL; ++i) {
        float d = dot2(Kr[s][i][0], qv[0], 0.f);
        d = dot2(Kr[s][i][1], qv[1], d);
        d = dot2(Kr[s][i][2], qv[2], d);
        d = dot2(Kr[s][i][3], qv[3], d);
        x[i] = d;
      }
      if (more) {
#pragma unroll
        for (int i = 0; i < TL; ++i) Kr[s][i] = ldnt(kb + (t0 + DEPTH + s) * TSTRIDE + i * 64);
      }
#pragma unroll
      for (int i = 0; i < TL; ++i) x[i] = row16_sum(x[i]) * C2;
      float mt = x[0];
#pragma unroll
      for (int i = 1; i < TL; ++i) mt = fmaxf(mt, x[i]);
      const float mn = fmaxf(m, mt);
      const float alpha = __builtin_amdgcn_exp2f(m - mn);
      m = mn;
      float p[TL], ps = 0.f;
#pragma unroll
      for (int i = 0; i < TL; ++i) { p[i] = __builtin_amdgcn_exp2f(x[i] - mn); ps += p[i]; }
      l = fmaf(l, alpha, ps);
#pragma unroll
      for (int j = 0; j < 8; ++j) acc[j] *= alpha;
#pragma unroll
      for (int i = 0; i < TL; ++i) {
#pragma unroll
        for (int j = 0; j < 4; ++j) {
          acc[2 * j] = fmaf(p[i], __builtin_bit_cast(float, Vr[s][i][j] << 16), acc[2 * j]);
          acc[2 * j + 1] = fmaf(p[i], __builtin_bit_cast(float, Vr[s][i][j] & 0xffff0000u), acc[2 * j + 1]);
        }
      }
      if (more) {
#pragma unroll
        for (int i = 0; i < TL; ++i) Vr[s][i] = ldnt(vb + (t0 + DEPTH + s) * TSTRIDE + i * 64);
      }
    }
  }

  const float mw = xrow_max(m);
  const float wr = __builtin_amdgcn_exp2f(m - mw);
  l = xrow_sum(l * wr);
#pragma unroll
  for (int j = 0; j < 8; ++j) acc[j] = xrow_sum(acc[j] * wr);
  float* rec = lds + wave * (D + 4);
  if (r == 0) {
    *reinterpret_cast<f32x4*>(rec + 4 + 8 * c) = f32x4{acc[0], acc[1], acc[2], acc[3]};
    *reinterpret_cast<f32x4*>(rec + 8 + 8 * c) = f32x4{acc[4], acc[5], acc[6], acc[7]};
    if (c == 0) { rec[0] = mw; rec[1] = l; }
  }
  __syncthreads();
  if (wave == 0) {
    float M = -INFINITY;
#pragma unroll
    for (int w = 0; w < NW; ++w) M = fmaxf(M, lds[w * (D + 4)]);
    float L = 0.f, o0 = 0.f, o1 = 0.f;
#pragma unroll
    for (int w = 0; w < NW; ++w) {
      const float* rw = lds + w * (D + 4);
      const float wt = __builtin_amdgcn_exp2f(rw[0] - M);
      L = fmaf(wt, rw[1], L);
      o0 = fmaf(wt, rw[4 + 2 * lane], o0);
      o1 = fmaf(wt, rw[5 + 2 * lane], o1);
    }
    const float inv = 1.f / L;
    const __bf16 b0 = (__bf16)(o0 * inv), b1 = (__bf16)(o1 * inv);
    const unsigned packed = (unsigned)__builtin_bit_cast(unsigned short, b0) | ((unsigned)__builtin_bit_cast(unsigned short, b1) << 16);
    out[head * 64 + lane] = packed;
  }
}

extern "C" void kernel_launch(void* const* d_in, const int* in_sizes, int n_in, void* d_out, int out_size, void* d_ws,
                              size_t ws_size, hipStream_t stream) {
  const int nhead = in_sizes[0] / D;
  attn_decode_kernel<<<nhead, NW * 64, 0, stream>>>((const u32x4*)d_in[0], (const u32x4*)d_in[1], (const u32x4*)d_in[2],
                                                    (unsigned*)d_out);
}
```

```cpp
#include <hip/hip_runtime.h>
#include <hip/hip_bf16.h>
#include <stdint.h>

typedef unsigned u32x4 __attribute__((ext_vector_type(4)));
typedef float f32x4 __attribute__((ext_vector_type(4)));
typedef __bf16 bf16x2 __attribute__((ext_vector_type(2)));
typedef __bf16 bf16x8 __attribute__((ext_vector_type(8)));

constexpr int D = 128, SKV = 4096;
constexpr int NW = 8;
constexpr int NG = SKV / 16 / NW;
constexpr int TBG = 3;
constexpr int AUX = 18;
constexpr unsigned HEAD_BYTES = SKV * D * 2;
constexpr unsigned GSTRIDE = NW * 4096;
constexpr int SC_WORDS = NG * 16;
constexpr int LO_OFF = SC_WORDS + 16;
constexpr int WREG = LO_OFF + SC_WORDS;
constexpr int REC = D + 4;
static_assert(SC_WORDS % 256 == 0 && NG > TBG + 1, "layout");

__host__ __device__ constexpr int issue_n(int i) { return i == 2 ? 0 : i == 3 ? 2 : 1; }
__host__ __device__ constexpr int issue_first(int i) { return i == 0 ? 3 : i == 1 ? 4 : 5; }

__device__ __forceinline__ float dot2(unsigned k, unsigned q, float acc) {
  return __builtin_amdgcn_fdot2_f32_bf16(__builtin_bit_cast(bf16x2, k), __builtin_bit_cast(bf16x2, q), acc, false);
}
template <int CTRL> __device__ __forceinline__ float dpp(float x) {
  return __builtin_bit_cast(float, __builtin_amdgcn_mov_dpp(__builtin_bit_cast(int, x), CTRL, 0xf, 0xf, true));
}
constexpr int XOR1 = 0xB1, XOR2 = 0x4E, XOR7 = 0x141, XOR8 = 0x128;
__device__ __forceinline__ float wave_max(float x) {
  x = fmaxf(x, dpp<XOR1>(x)); x = fmaxf(x, dpp<XOR2>(x)); x = fmaxf(x, dpp<XOR7>(x)); x = fmaxf(x, dpp<XOR8>(x));
  auto s = __builtin_amdgcn_permlane16_swap(__float_as_uint(x), __float_as_uint(x), false, false);
  x = fmaxf(__uint_as_float(s[0]), __uint_as_float(s[1]));
  auto t = __builtin_amdgcn_permlane32_swap(__float_as_uint(x), __float_as_uint(x), false, false);
  return fmaxf(__uint_as_float(t[0]), __uint_as_float(t[1]));
}
__device__ __forceinline__ float wave_sum(float x) {
  x += dpp<XOR1>(x); x += dpp<XOR2>(x); x += dpp<XOR7>(x); x += dpp<XOR8>(x);
  auto s = __builtin_amdgcn_permlane16_swap(__float_as_uint(x), __float_as_uint(x), false, false);
  x = __uint_as_float(s[0]) + __uint_as_float(s[1]);
  auto t = __builtin_amdgcn_permlane32_swap(__float_as_uint(x), __float_as_uint(x), false, false);
  return __uint_as_float(t[0]) + __uint_as_float(t[1]);
}
__device__ __forceinline__ float transpose_reduce4(float x0, float x1, float x2, float x3) {
  asm volatile(
      "s_nop 2\n\t"
      "v_add_f32_dpp %0, %0, %0 row_ror:8 row_mask:0xf bank_mask:0x3\n\t"
      "v_add_f32_dpp %1, %1, %1 row_ror:8 row_mask:0xf bank_mask:0x3\n\t"
      "v_add_f32_dpp %0, %2, %2 row_ror:8 row_mask:0xf bank_mask:0xc\n\t"
      "v_add_f32_dpp %1, %3, %3 row_ror:8 row_mask:0xf bank_mask:0xc\n\t"
      "s_nop 1\n\t"
      "v_add_f32_dpp %0, %0, %0 row_half_mirror row_mask:0xf bank_mask:0x5\n\t"
      "v_add_f32_dpp %0, %1, %1 row_half_mirror row_mask:0xf bank_mask:0xa\n\t"
      "s_nop 1\n\t"
      "v_add_f32_dpp %0, %0, %0 quad_perm:[1,0,3,2] row_mask:0xf bank_mask:0xf\n\t"
      "s_nop 1\n\t"
      "v_add_f32_dpp %0, %0, %0 quad_perm:[2,3,0,1] row_mask:0xf bank_mask:0xf\n\t"
      : "+v"(x0), "+v"(x1) : "v"(x2), "v"(x3));
  return x0;
}
__device__ __forceinline__ unsigned pack2(float a) {
  const bf16x2 r = {(__bf16)a, (__bf16)a};
  return __builtin_bit_cast(unsigned, r);
}

#define PIN(X) asm volatile("" : "+v"(X) :: "memory")
#define LD(RS, SO, I) __builtin_amdgcn_raw_buffer_load_b128(RS, voff + (I) * 1024, SO, AUX)

__global__ void __launch_bounds__(NW * 64) attn_decode_kernel(const u32x4* __restrict__ q, const char* __restrict__ k,
                                                              const char* __restrict__ v, unsigned* __restrict__ out) {
  __shared__ __attribute__((aligned(16))) float lds[NW * WREG + NW * REC];
  const unsigned t_s = (unsigned)__builtin_amdgcn_s_memrealtime();
  unsigned xcc; asm volatile("s_getreg_b32 %0, hwreg(HW_REG_XCC_ID)" : "=s"(xcc));
  const int head = blockIdx.x;
  const int tid = threadIdx.x, lane = tid & 63, wave = __builtin_amdgcn_readfirstlane(tid >> 6);
  const int r = lane >> 4, c = lane & 15;
  constexpr float C2 = 0.088388347648318440f * 1.4426950408889634f;

  const char* kh = k + (size_t)head * HEAD_BYTES;
  const char* vh = v + (size_t)head * HEAD_BYTES;
  const auto rk = __builtin_amdgcn_make_buffer_rsrc((void*)kh, 0, HEAD_BYTES, 0x00020000);
  const auto rv = __builtin_amdgcn_make_buffer_rsrc((void*)vh, 0, HEAD_BYTES, 0x00020000);
  const unsigned voff = lane * 16;
  const unsigned so0 = wave * 4096;

  const u32x4 qv = q[head * 16 + c];
  u32x4 R[4];
#pragma unroll
  for (int i = 0; i < 3; ++i) R[i] = LD(rk, so0, i);
  __builtin_amdgcn_sched_barrier(0);

  float* sc = lds + wave * WREG;

  unsigned so = so0;
  for (int g = 0; g < NG; ++g) {
    const bool last = g == NG - 1;
    const auto rn = __builtin_amdgcn_make_buffer_rsrc((void*)(last ? vh : kh), 0, HEAD_BYTES, 0x00020000);
    const unsigned sn = last ? so0 : so + GSTRIDE;
    float x[4];
#pragma unroll
    for (int i = 0; i < 4; ++i) {
      PIN(R[i]);
#pragma unroll
      for (int j = issue_first(i); j < issue_first(i) + issue_n(i); ++j) {
        if (j < 4) R[j] = LD(rk, so, j); else R[j - 4] = LD(rn, sn, j - 4);
      }
      __builtin_amdgcn_sched_barrier(0);
      float d = dot2(R[i][0], qv[0], 0.f);
      d = dot2(R[i][1], qv[1], d);
      d = dot2(R[i][2], qv[2], d);
      x[i] = dot2(R[i][3], qv[3], d);
    }
    sc[g * 16 + r * 4 + (c >> 2)] = transpose_reduce4(x[0], x[1], x[2], x[3]);
    so = sn;
  }
  asm volatile("" ::: "memory");

  float m, l;
  {
    constexpr int NP = SC_WORDS / 256;
    f32x4 sv[NP];
    float mx = -INFINITY;
#pragma unroll
    for (int j = 0; j < NP; ++j) {
      sv[j] = *reinterpret_cast<const f32x4*>(sc + j * 256 + lane * 4);
      mx = fmaxf(fmaxf(mx, fmaxf(sv[j][0], sv[j][1])), fmaxf(sv[j][2], sv[j][3]));
    }
    mx = wave_max(mx);
    m = mx * C2;
    const float ms = (mx == -INFINITY) ? 0.f : m;
    float ls = 0.f;
#pragma unroll
    for (int j = 0; j < NP; ++j) {
      u32x4 hi, lo;
#pragma unroll
      for (int e = 0; e < 4; ++e) {
        const float p = __builtin_amdgcn_exp2f(fmaf(sv[j][e], C2, -ms));
        ls += p;
        hi[e] = pack2(p);
        lo[e] = pack2(p - __builtin_bit_cast(float, hi[e] & 0xffff0000u));
      }
      *reinterpret_cast<u32x4*>(sc + j * 256 + lane * 4) = hi;
      *reinterpret_cast<u32x4*>(sc + LO_OFF + j * 256 + lane * 4) = lo;
    }
    l = wave_sum(ls);
  }
  asm volatile("" ::: "memory");

  unsigned mask[4];
#pragma unroll
  for (int w = 0; w < 4; ++w) mask[w] = (((c & 7) >> 1) == w) ? ((c & 1) ? 0xffff0000u : 0x0000ffffu) : 0u;
  const float* pp = sc + (c >= 8 ? LO_OFF : 0) + r * 4;
  f32x4 acc = {0.f, 0.f, 0.f, 0.f};
  u32x4 pw = *reinterpret_cast<const u32x4*>(pp);
  for (int g = 0; g < NG - TBG - 1; ++g) {
    const unsigned sn = so + GSTRIDE;
    const u32x4 pc = pw;
    pw = *reinterpret_cast<const u32x4*>(pp + (g + 1) * 16);
#pragma unroll
    for (int i = 0; i < 4; ++i) {
      u32x4 b = {pc[i] & mask[0], pc[i] & mask[1], pc[i] & mask[2], pc[i] & mask[3]};
      PIN(b);
      PIN(R[i]);
#pragma unroll
      for (int j = issue_first(i); j < issue_first(i) + issue_n(i); ++j) {
        if (j < 4) R[j] = LD(rv, so, j); else R[j - 4] = LD(rv, sn, j - 4);
      }
      __builtin_amdgcn_sched_barrier(0);
      acc = __builtin_amdgcn_mfma_f32_16x16x32_bf16(__builtin_bit_cast(bf16x8, R[i]), __builtin_bit_cast(bf16x8, b), acc, 0, 0, 0);
    }
    so = sn;
  }
  {
    u32x4 T[TBG * 4];
    R[3] = LD(rv, so, 3);
    __builtin_amdgcn_sched_barrier(0);
#pragma unroll
    for (int t = 0; t < TBG * 4; ++t) {
      T[t] = LD(rv, so + (1 + (t >> 2)) * GSTRIDE, t & 3);
      __builtin_amdgcn_sched_barrier(0);
    }
    u32x4 pt[TBG + 1];
    pt[0] = pw;
#pragma unroll
    for (int t = 0; t < TBG; ++t) pt[t + 1] = *reinterpret_cast<const u32x4*>(pp + (NG - TBG + t) * 16);
#pragma unroll
    for (int j = 0; j < (TBG + 1) * 4; ++j) {
      const unsigned pj = pt[j >> 2][j & 3];
      const u32x4 b = {pj & mask[0], pj & mask[1], pj & mask[2], pj & mask[3]};
      const u32x4 a = j < 4 ? R[j & 3] : T[j < 4 ? 0 : j - 4];
      acc = __builtin_amdgcn_mfma_f32_16x16x32_bf16(__builtin_bit_cast(bf16x8, a), __builtin_bit_cast(bf16x8, b), acc, 0, 0, 0);
    }
  }

  float* rec = lds + NW * WREG + wave * REC;
  f32x4 o4;
#pragma unroll
  for (int e = 0; e < 4; ++e) o4[e] = acc[e] + dpp<XOR8>(acc[e]);
  if (c < 8) *reinterpret_cast<f32x4*>(rec + 4 + (r * 8 + c) * 4) = o4;
  if (lane == 0) { rec[0] = m; rec[1] = l; }
  __syncthreads();
  if (wave == 0) {
    const float* recs = lds + NW * WREG;
    const int idx = 4 + ((lane >> 4) * 8 + 2 * (lane & 3)) * 4 + ((lane >> 2) & 3);
    float mw[NW], lw[NW], a0[NW], a1[NW];
#pragma unroll
    for (int w = 0; w < NW; ++w) {
      const float* rw = recs + w * REC;
      mw[w] = rw[0]; lw[w] = rw[1]; a0[w] = rw[idx]; a1[w] = rw[idx + 4];
    }
    __builtin_amdgcn_sched_barrier(0);
    float M = -INFINITY;
#pragma unroll
    for (int w = 0; w < NW; ++w) M = fmaxf(M, mw[w]);
    float L = 0.f, o0 = 0.f, o1 = 0.f;
#pragma unroll
    for (int w = 0; w < NW; ++w) {
      const float wt = __builtin_amdgcn_exp2f(mw[w] - M);
      L = fmaf(wt, lw[w], L);
      o0 = fmaf(wt, a0[w], o0);
      o1 = fmaf(wt, a1[w], o1);
    }
    const float inv = 1.f / L;
    const bf16x2 o = {(__bf16)(o0 * inv), (__bf16)(o1 * inv)};
    out[head * 64 + lane] = __builtin_bit_cast(unsigned, o);
    if ((xcc & 7) == 0) { while ((unsigned)__builtin_amdgcn_s_memrealtime() - t_s < 15000u) __builtin_amdgcn_s_sleep(2); }
  }
}

extern "C" void kernel_launch(void* const* d_in, const int* in_sizes, int n_in, void* d_out, int out_size, void* d_ws,
                              size_t ws_size, hipStream_t stream) {
  const int nhead = in_sizes[0] / D;
  attn_decode_kernel<<<nhead, NW * 64, 0, stream>>>((const u32x4*)d_in[0], (const char*)d_in[1], (const char*)d_in[2],
                                                    (unsigned*)d_out);
}
```

```cpp
#include <hip/hip_runtime.h>
#include <hip/hip_bf16.h>
#include <stdint.h>

typedef unsigned u32x4 __attribute__((ext_vector_type(4)));
typedef float f32x4 __attribute__((ext_vector_type(4)));
typedef __bf16 bf16x2 __attribute__((ext_vector_type(2)));
typedef __bf16 bf16x8 __attribute__((ext_vector_type(8)));

constexpr int D = 128, SKV = 4096;
constexpr int NW = 8;
constexpr int NG = SKV / 16 / NW;
constexpr int TBG = 5;
constexpr unsigned GATE = 6670;
constexpr int AUX = 18;
constexpr unsigned HEAD_BYTES = SKV * D * 2;
constexpr unsigned GSTRIDE = NW * 4096;
constexpr int SC_WORDS = NG * 16;
constexpr int LO_OFF = SC_WORDS + 16;
constexpr int WREG = LO_OFF + SC_WORDS;
constexpr int REC = D + 4;
static_assert(SC_WORDS % 256 == 0 && NG > TBG + 1, "layout");

__host__ __device__ constexpr int issue_n(int i) { return i == 2 ? 0 : i == 3 ? 2 : 1; }
__host__ __device__ constexpr int issue_first(int i) { return i == 0 ? 3 : i == 1 ? 4 : 5; }

__device__ __forceinline__ float dot2(unsigned k, unsigned q, float acc) {
  return __builtin_amdgcn_fdot2_f32_bf16(__builtin_bit_cast(bf16x2, k), __builtin_bit_cast(bf16x2, q), acc, false);
}
template <int CTRL> __device__ __forceinline__ float dpp(float x) {
  return __builtin_bit_cast(float, __builtin_amdgcn_mov_dpp(__builtin_bit_cast(int, x), CTRL, 0xf, 0xf, true));
}
constexpr int XOR1 = 0xB1, XOR2 = 0x4E, XOR7 = 0x141, XOR8 = 0x128;
__device__ __forceinline__ float wave_max(float x) {
  x = fmaxf(x, dpp<XOR1>(x)); x = fmaxf(x, dpp<XOR2>(x)); x = fmaxf(x, dpp<XOR7>(x)); x = fmaxf(x, dpp<XOR8>(x));
  auto s = __builtin_amdgcn_permlane16_swap(__float_as_uint(x), __float_as_uint(x), false, false);
  x = fmaxf(__uint_as_float(s[0]), __uint_as_float(s[1]));
  auto t = __builtin_amdgcn_permlane32_swap(__float_as_uint(x), __float_as_uint(x), false, false);
  return fmaxf(__uint_as_float(t[0]), __uint_as_float(t[1]));
}
__device__ __forceinline__ float wave_sum(float x) {
  x += dpp<XOR1>(x); x += dpp<XOR2>(x); x += dpp<XOR7>(x); x += dpp<XOR8>(x);
  auto s = __builtin_amdgcn_permlane16_swap(__float_as_uint(x), __float_as_uint(x), false, false);
  x = __uint_as_float(s[0]) + __uint_as_float(s[1]);
  auto t = __builtin_amdgcn_permlane32_swap(__float_as_uint(x), __float_as_uint(x), false, false);
  return __uint_as_float(t[0]) + __uint_as_float(t[1]);
}
__device__ __forceinline__ float transpose_reduce4(float x0, float x1, float x2, float x3) {
  asm volatile(
      "s_nop 2\n\t"
      "v_add_f32_dpp %0, %0, %0 row_ror:8 row_mask:0xf bank_mask:0x3\n\t"
      "v_add_f32_dpp %1, %1, %1 row_ror:8 row_mask:0xf bank_mask:0x3\n\t"
      "v_add_f32_dpp %0, %2, %2 row_ror:8 row_mask:0xf bank_mask:0xc\n\t"
      "v_add_f32_dpp %1, %3, %3 row_ror:8 row_mask:0xf bank_mask:0xc\n\t"
      "s_nop 1\n\t"
      "v_add_f32_dpp %0, %0, %0 row_half_mirror row_mask:0xf bank_mask:0x5\n\t"
      "v_add_f32_dpp %0, %1, %1 row_half_mirror row_mask:0xf bank_mask:0xa\n\t"
      "s_nop 1\n\t"
      "v_add_f32_dpp %0, %0, %0 quad_perm:[1,0,3,2] row_mask:0xf bank_mask:0xf\n\t"
      "s_nop 1\n\t"
      "v_add_f32_dpp %0, %0, %0 quad_perm:[2,3,0,1] row_mask:0xf bank_mask:0xf\n\t"
      : "+v"(x0), "+v"(x1) : "v"(x2), "v"(x3));
  return x0;
}
__device__ __forceinline__ unsigned pack2(float a) {
  const bf16x2 r = {(__bf16)a, (__bf16)a};
  return __builtin_bit_cast(unsigned, r);
}

#define PIN(X) asm volatile("" : "+v"(X) :: "memory")
#define LD(RS, SO, I) __builtin_amdgcn_raw_buffer_load_b128(RS, voff + (I) * 1024, SO, AUX)

__global__ void __launch_bounds__(NW * 64) attn_decode_kernel(const u32x4* __restrict__ q, const char* __restrict__ k,
                                                              const char* __restrict__ v, unsigned* __restrict__ out) {
  __shared__ __attribute__((aligned(16))) float lds[NW * WREG + NW * REC];
  const int head = blockIdx.x;
  const int tid = threadIdx.x, lane = tid & 63, wave = __builtin_amdgcn_readfirstlane(tid >> 6);
  const int r = lane >> 4, c = lane & 15;
  constexpr float C2 = 0.088388347648318440f * 1.4426950408889634f;

  const char* kh = k + (size_t)head * HEAD_BYTES;
  const char* vh = v + (size_t)head * HEAD_BYTES;
  const auto rk = __builtin_amdgcn_make_buffer_rsrc((void*)kh, 0, HEAD_BYTES, 0x00020000);
  const auto rv = __builtin_amdgcn_make_buffer_rsrc((void*)vh, 0, HEAD_BYTES, 0x00020000);
  const unsigned voff = lane * 16;
  const unsigned so0 = wave * 4096;

  const u32x4 qv = q[head * 16 + c];
  u32x4 R[4];
#pragma unroll
  for (int i = 0; i < 3; ++i) R[i] = LD(rk, so0, i);
  __builtin_amdgcn_sched_barrier(0);
  const unsigned t_gate = (unsigned)__builtin_amdgcn_s_memrealtime() + GATE;

  float* sc = lds + wave * WREG;

  unsigned so = so0;
  for (int g = 0; g < NG; ++g) {
    const bool last = g == NG - 1;
    const auto rn = __builtin_amdgcn_make_buffer_rsrc((void*)(last ? vh : kh), 0, HEAD_BYTES, 0x00020000);
    const unsigned sn = last ? so0 : so + GSTRIDE;
    float x[4];
#pragma unroll
    for (int i = 0; i < 4; ++i) {
      PIN(R[i]);
#pragma unroll
      for (int j = issue_first(i); j < issue_first(i) + issue_n(i); ++j) {
        if (j < 4) R[j] = LD(rk, so, j); else R[j - 4] = LD(rn, sn, j - 4);
      }
      __builtin_amdgcn_sched_barrier(0);
      float d = dot2(R[i][0], qv[0], 0.f);
      d = dot2(R[i][1], qv[1], d);
      d = dot2(R[i][2], qv[2], d);
      x[i] = dot2(R[i][3], qv[3], d);
    }
    sc[g * 16 + r * 4 + (c >> 2)] = transpose_reduce4(x[0], x[1], x[2], x[3]);
    so = sn;
  }
  asm volatile("" ::: "memory");

  float m, l;
  {
    constexpr int NP = SC_WORDS / 256;
    f32x4 sv[NP];
    float mx = -INFINITY;
#pragma unroll
    for (int j = 0; j < NP; ++j) {
      sv[j] = *reinterpret_cast<const f32x4*>(sc + j * 256 + lane * 4);
      mx = fmaxf(fmaxf(mx, fmaxf(sv[j][0], sv[j][1])), fmaxf(sv[j][2], sv[j][3]));
    }
    mx = wave_max(mx);
    m = mx * C2;
    const float ms = (mx == -INFINITY) ? 0.f : m;
    float ls = 0.f;
#pragma unroll
    for (int j = 0; j < NP; ++j) {
      u32x4 hi, lo;
#pragma unroll
      for (int e = 0; e < 4; ++e) {
        const float p = __builtin_amdgcn_exp2f(fmaf(sv[j][e], C2, -ms));
        ls += p;
        hi[e] = pack2(p);
        lo[e] = pack2(p - __builtin_bit_cast(float, hi[e] & 0xffff0000u));
      }
      *reinterpret_cast<u32x4*>(sc + j * 256 + lane * 4) = hi;
      *reinterpret_cast<u32x4*>(sc + LO_OFF + j * 256 + lane * 4) = lo;
    }
    l = wave_sum(ls);
  }
  asm volatile("" ::: "memory");

  unsigned mask[4];
#pragma unroll
  for (int w = 0; w < 4; ++w) mask[w] = (((c & 7) >> 1) == w) ? ((c & 1) ? 0xffff0000u : 0x0000ffffu) : 0u;
  const float* pp = sc + (c >= 8 ? LO_OFF : 0) + r * 4;
  f32x4 acc = {0.f, 0.f, 0.f, 0.f};
  u32x4 pw = *reinterpret_cast<const u32x4*>(pp);
  int g = 0;
  bool more;
  do {
    const unsigned sn = so + GSTRIDE;
    const u32x4 pc = pw;
    pw = *reinterpret_cast<const u32x4*>(pp + (g + 1) * 16);
#pragma unroll
    for (int i = 0; i < 4; ++i) {
      u32x4 b = {pc[i] & mask[0], pc[i] & mask[1], pc[i] & mask[2], pc[i] & mask[3]};
      PIN(b);
      PIN(R[i]);
#pragma unroll
      for (int j = issue_first(i); j < issue_first(i) + issue_n(i); ++j) {
        if (j < 4) R[j] = LD(rv, so, j); else R[j - 4] = LD(rv, sn, j - 4);
      }
      __builtin_amdgcn_sched_barrier(0);
      acc = __builtin_amdgcn_mfma_f32_16x16x32_bf16(__builtin_bit_cast(bf16x8, R[i]), __builtin_bit_cast(bf16x8, b), acc, 0, 0, 0);
    }
    so = sn;
    ++g;
    more = g < NG - TBG - 1;
    if (!more && g < NG - 1) more = (int)((unsigned)__builtin_amdgcn_s_memrealtime() - t_gate) < 0;
  } while (more);
  {
    u32x4 T[TBG * 4];
    R[3] = LD(rv, so, 3);
    __builtin_amdgcn_sched_barrier(0);
#pragma unroll
    for (int t = 0; t < TBG; ++t) {
      const unsigned st = (NG - TBG + t > g) ? so0 + (NG - TBG + t) * GSTRIDE : HEAD_BYTES;
#pragma unroll
      for (int i = 0; i < 4; ++i) { T[t * 4 + i] = LD(rv, st, i); __builtin_amdgcn_sched_barrier(0); }
    }
    u32x4 pt[TBG];
#pragma unroll
    for (int t = 0; t < TBG; ++t) pt[t] = *reinterpret_cast<const u32x4*>(pp + (NG - TBG + t) * 16);
#pragma unroll
    for (int i = 0; i < 4; ++i) {
      u32x4 b = {pw[i] & mask[0], pw[i] & mask[1], pw[i] & mask[2], pw[i] & mask[3]};
      asm volatile("; tail head" : "+v"(b));
      acc = __builtin_amdgcn_mfma_f32_16x16x32_bf16(__builtin_bit_cast(bf16x8, R[i]), __builtin_bit_cast(bf16x8, b), acc, 0, 0, 0);
    }
#pragma unroll
    for (int t = 0; t < TBG; ++t) {
      if (NG - TBG + t > g) {
#pragma unroll
        for (int i = 0; i < 4; ++i) {
          const u32x4 b = {pt[t][i] & mask[0], pt[t][i] & mask[1], pt[t][i] & mask[2], pt[t][i] & mask[3]};
          acc = __builtin_amdgcn_mfma_f32_16x16x32_bf16(__builtin_bit_cast(bf16x8, T[t * 4 + i]), __builtin_bit_cast(bf16x8, b), acc, 0, 0, 0);
        }
      }
    }
#pragma unroll
    for (int t = TBG * 4 - 1; t >= 0; --t) asm volatile("" :: "v"(T[t]));
#pragma unroll
    for (int t = TBG - 1; t >= 0; --t) asm volatile("" :: "v"(pt[t]));
  }

  float* rec = lds + NW * WREG + wave * REC;
  f32x4 o4;
#pragma unroll
  for (int e = 0; e < 4; ++e) o4[e] = acc[e] + dpp<XOR8>(acc[e]);
  if (c < 8) *reinterpret_cast<f32x4*>(rec + 4 + (r * 8 + c) * 4) = o4;
  if (lane == 0) { rec[0] = m; rec[1] = l; }
  __syncthreads();
  if (wave == 0) {
    const float* recs = lds + NW * WREG;
    const int idx = 4 + ((lane >> 4) * 8 + 2 * (lane & 3)) * 4 + ((lane >> 2) & 3);
    float mw[NW], lw[NW], a0[NW], a1[NW];
#pragma unroll
    for (int w = 0; w < NW; ++w) {
      const float* rw = recs + w * REC;
      mw[w] = rw[0]; lw[w] = rw[1]; a0[w] = rw[idx]; a1[w] = rw[idx + 4];
    }
    __builtin_amdgcn_sched_barrier(0);
    float M = -INFINITY;
#pragma unroll
    for (int w = 0; w < NW; ++w) M = fmaxf(M, mw[w]);
    float L = 0.f, o0 = 0.f, o1 = 0.f;
#pragma unroll
    for (int w = 0; w < NW; ++w) {
      const float wt = __builtin_amdgcn_exp2f(mw[w] - M);
      L = fmaf(wt, lw[w], L);
      o0 = fmaf(wt, a0[w], o0);
      o1 = fmaf(wt, a1[w], o1);
    }
    const float inv = 1.f / L;
    const bf16x2 o = {(__bf16)(o0 * inv), (__bf16)(o1 * inv)};
    out[head * 64 + lane] = __builtin_bit_cast(unsigned, o);
  }
}

extern "C" void kernel_launch(void* const* d_in, const int* in_sizes, int n_in, void* d_out, int out_size, void* d_ws,
                              size_t ws_size, hipStream_t stream) {
  const int nhead = in_sizes[0] / D;
  attn_decode_kernel<<<nhead, NW * 64, 0, stream>>>((const u32x4*)d_in[0], (const char*)d_in[1], (const char*)d_in[2],
                                                    (unsigned*)d_out);
}
```
